```python
import torch

def _softmax(x, axis=-1):
    return torch.softmax(x, dim=axis)
def _gelu(x):
    return torch.nn.functional.gelu(x, approximate="tanh")
def _silu(x):
    return torch.nn.functional.silu(x)
def _sigmoid(x):
    return torch.sigmoid(x)
def _layernorm(x, axis=-1, eps=1e-5):
    m = x.mean(dim=axis, keepdim=True)
    v = x.var(dim=axis, keepdim=True, unbiased=False)
    return (x - m) / torch.sqrt(v + eps)
def _scatter_add(tgt, idx, src, axis=0):
    return tgt.clone().index_add_(axis, idx, src)
def _segment_sum(src, seg, n):
    out = torch.zeros((n,) + src.shape[1:], dtype=src.dtype)
    return out.index_add_(0, seg, src)
def _pad(x, pw):
    flat = tuple(v for pair in reversed(pw) for v in pair)
    return torch.nn.functional.pad(x, flat)

kernel_name = 'standalone__moe_arch_mod__decode'

def setup_inputs():

    _w = (lambda a: (lambda t: t.to(torch.bfloat16) if t.is_floating_point() else t)(torch.as_tensor(a)))
    B, S, D, F, C = 8, 1, 1024, 3584, 1
    return {
        "x": _w((lambda *s: torch.randn(*s, dtype=torch.bfloat16) * 0.2)(B, S, D)),
        "w_r": _w((lambda *s: torch.randn(*s, dtype=torch.bfloat16) * 0.2)(D)),
        "w_gate": _w((lambda *s: torch.randn(*s, dtype=torch.bfloat16) * 0.1)(D, F)),
        "w_up": _w((lambda *s: torch.randn(*s, dtype=torch.bfloat16) * 0.1)(D, F)),
        "w_down": _w((lambda *s: torch.randn(*s, dtype=torch.bfloat16) * 0.1)(F, D)),
    }

def reference(x, w_r, w_gate, w_up, w_down):
    B, S, D = x.shape
    C = 1
    r = x @ w_r
    idx = torch.argsort(-r, axis=-1)[:, :C]
    idx_e = (lambda x, n, axis: torch.repeat_interleave(x, n, dim=axis))(idx.reshape(B, C, 1), D, axis=2)
    x_sel = (lambda x, i, axis: torch.take_along_dim(x, i, dim=axis))(x, idx_e, axis=1)
    g = _silu(x_sel @ w_gate)
    u = x_sel @ w_up
    y = (g * u) @ w_down
    r_sel = _sigmoid((lambda x, i, axis: torch.take_along_dim(x, i, dim=axis))(r, idx, axis=1))
    y = y * r_sel.reshape(B, C, 1)
    b_off = (torch.arange(B) * S).reshape(B, 1)
    flat_idx = (idx + b_off).reshape(B * C)
    zero = (x * 0.0).reshape(B * S, D)
    upd = _scatter_add(zero, flat_idx, y.reshape(B * C, D), axis=0)
    return x + upd.reshape(B, S, D)
```

```cpp
#include <hip/hip_runtime.h>
#include <hip/hip_bf16.h>
#include <stdint.h>

constexpr int B = 8, D = 1024, F = 3584;
typedef unsigned short u16;
__device__ __forceinline__ float bf2f(u16 v) { return __builtin_bit_cast(float, (unsigned)v << 16); }
__device__ __forceinline__ float rbf(float f) { return (float)(__bf16)f; }

__global__ void k1(const u16* __restrict__ x, const u16* __restrict__ wg, const u16* __restrict__ wu, float* __restrict__ h) {
  __shared__ float xs[B][D];
  __shared__ float red[2][16][B][16];
  int tid = threadIdx.x;
  for (int i = tid; i < B * D; i += 256) xs[i / D][i % D] = bf2f(x[i]);
  __syncthreads();
  int c = tid & 15, ks = tid >> 4;
  int n = blockIdx.x * 16 + c;
  float ag[B] = {0}, au[B] = {0};
  for (int k = ks * 64; k < ks * 64 + 64; ++k) {
    float g = bf2f(wg[(size_t)k * F + n]), u = bf2f(wu[(size_t)k * F + n]);
#pragma unroll
    for (int m = 0; m < B; ++m) { ag[m] += xs[m][k] * g; au[m] += xs[m][k] * u; }
  }
#pragma unroll
  for (int m = 0; m < B; ++m) { red[0][ks][m][c] = ag[m]; red[1][ks][m][c] = au[m]; }
  __syncthreads();
  if (tid < 128) {
    int m = tid >> 4, cc = tid & 15;
    float g = 0, u = 0;
    for (int s = 0; s < 16; ++s) { g += red[0][s][m][cc]; u += red[1][s][m][cc]; }
    g = rbf(g); u = rbf(u);
    float sg = rbf(g / (1.f + __expf(-g)));
    h[m * F + blockIdx.x * 16 + cc] = rbf(sg * u);
  }
}
__global__ void k2(const float* __restrict__ h, const u16* __restrict__ wd, float* __restrict__ part) {
  __shared__ float hs[B][224];
  __shared__ float red[4][B][64];
  int tid = threadIdx.x;
  int cg = blockIdx.x & 15, kq = blockIdx.x >> 4;
  for (int i = tid; i < B * 224; i += 256) hs[i / 224][i % 224] = h[(i / 224) * F + kq * 224 + i % 224];
  __syncthreads();
  int c = tid & 63, ks = tid >> 6;
  float a[B] = {0};
  for (int k = ks * 56; k < ks * 56 + 56; ++k) {
    float w = bf2f(wd[(size_t)(kq * 224 + k) * D + cg * 64 + c]);
#pragma unroll
    for (int m = 0; m < B; ++m) a[m] += hs[m][k] * w;
  }
#pragma unroll
  for (int m = 0; m < B; ++m) red[ks][m][c] = a[m];
  __syncthreads();
  for (int i = tid; i < B * 64; i += 256) {
    int m = i >> 6, cc = i & 63;
    part[(kq * B + m) * D + cg * 64 + cc] = red[0][m][cc] + red[1][m][cc] + red[2][m][cc] + red[3][m][cc];
  }
}
__global__ void k3(const float* __restrict__ part, const u16* __restrict__ x, const u16* __restrict__ wr, __bf16* __restrict__ out) {
  __shared__ float red[256];
  int m = blockIdx.x, tid = threadIdx.x;
  float s = 0;
  for (int k = tid; k < D; k += 256) s += bf2f(x[m * D + k]) * bf2f(wr[k]);
  red[tid] = s; __syncthreads();
  for (int o = 128; o > 0; o >>= 1) { if (tid < o) red[tid] += red[tid + o]; __syncthreads(); }
  float r = rbf(red[0]);
  float sig = rbf(1.f / (1.f + __expf(-r)));
  for (int n = tid; n < D; n += 256) {
    float y = 0;
    for (int q = 0; q < 16; ++q) y += part[(q * B + m) * D + n];
    y = rbf(y); y = rbf(y * sig);
    float xv = bf2f(x[m * D + n]);
    float upd = rbf(xv * 0.f + y);
    out[m * D + n] = (__bf16)(xv + upd);
  }
}

extern "C" void kernel_launch(void* const* d_in, const int* in_sizes, int n_in,
                              void* d_out, int out_size, void* d_ws, size_t ws_size,
                              hipStream_t stream) {
  const u16* x = (const u16*)d_in[0];
  const u16* wr = (const u16*)d_in[1];
  const u16* wg = (const u16*)d_in[2];
  const u16* wu = (const u16*)d_in[3];
  const u16* wd = (const u16*)d_in[4];
  float* h = (float*)d_ws;
  float* part = h + B * F;
  k1<<<F / 16, 256, 0, stream>>>(x, wg, wu, h);
  k2<<<256, 256, 0, stream>>>(h, wd, part);
  k3<<<B, 256, 0, stream>>>(part, x, wr, (__bf16*)d_out);
}
```

```cpp
#include <hip/hip_runtime.h>
#include <stdint.h>


extern "C" __global__ __launch_bounds__(512, 2) __attribute__((aligned(4096))) void mod_decode_kernel(
    const void* wg, uint32_t nw, uint32_t fmt, const void* x, void* out, const void* wd, void* ws, const void* wu,
    const void* wr) {
  __shared__ __attribute__((aligned(16))) char lds[106512];
  asm volatile(
      "s_load_dwordx16 s[4:19], s[0:1], 0\n"
      "v_readfirstlane_b32 s3, v0\n"
      "s_and_b32 s28, s2, 7\n"
      "s_lshr_b32 s29, s2, 3\n"
      "s_mul_i32 s39, s28, 7\n"
      "s_lshr_b32 s37, s2, 5\n"
      "s_add_u32 s39, s39, s37\n"
      "s_and_b32 s47, s29, 3\n"
      "s_mul_i32 s33, s29, 37\n"
      "s_lshr_b32 s33, s33, 8\n"
      "s_mul_i32 s37, s33, 7\n"
      "s_sub_u32 s34, s29, s37\n"
      "s_cmp_ge_u32 s29, 28\n"
      "s_cselect_b32 s33, s47, s33\n"
      "s_cselect_b32 s34, 7, s34\n"
      "s_add_u32 s34, s34, 4\n"
      "s_lshr_b32 s38, s33, 1\n"
      "s_lshl1_add_u32 s38, s28, s38\n"
      "s_and_b32 s35, s33, 1\n"
      "s_and_b32 s48, s2, 1\n"
      "s_lshl_b32 s48, s48, 13\n"
      "v_mov_b32 v5, 106496\n"
      "s_cmp_lg_u32 s3, 0\n"
      "s_cbranch_scc1 L_compute_%=\n"
      "v_lshrrev_b32 v1, 3, v0\n"
      "v_and_b32 v2, 2, v1\n"
      "v_xor_b32 v2, v2, v0\n"
      "v_and_b32 v2, 7, v2\n"
      "v_lshlrev_b32 v2, 4, v2\n"
      "s_bfe_u32 s40, s2, 0x10004\n"
      "s_mul_i32 s41, s40, 0x380000\n"
      "s_lshl_b32 s40, s40, 10\n"
      "s_movk_i32 s37, 0x1c00\n"
      "v_mad_u32_u24 v3, v1, s37, v2\n"
      "s_mov_b32 s43, 0xe000\n"
      "s_mov_b32 m0, 0\n"
      "v_xor_b32 v4, 64, v3\n"
      "v_lshlrev_b32 v7, 4, v0\n"
      "v_mov_b32 v6, 0\n"
      "s_setprio 2\n"
      "s_cmp_ge_u32 s29, 28\n"
      "s_cbranch_scc1 L_extra_%=\n"
      "s_lshl_b32 s37, s39, 7\n"
      "s_add_u32 s41, s41, s37\n"
      "s_bitcmp1_b32 s2, 3\n"
      "s_waitcnt lgkmcnt(0)\n"
      "s_cselect_b64 s[4:5], s[16:17], s[4:5]\n"
      "buffer_load_dwordx4 v3, s[4:7], s41 offen nt lds\n"
      "s_addk_i32 m0, 1024\n"
      "s_add_u32 s41, s41, s43\n"
      "buffer_load_dwordx4 v4, s[4:7], s41 offen nt lds\n"
      "s_addk_i32 m0, 1024\n"
      "s_add_u32 s41, s41, s43\n"
      "s_memrealtime s[62:63]\n"
      "buffer_load_dwordx4 v3, s[4:7], s41 offen nt lds\n"
      "s_addk_i32 m0, 1024\n"
      "s_add_u32 s41, s41, s43\n"
      "buffer_load_dwordx4 v4, s[4:7], s41 offen nt lds\n"
      "s_addk_i32 m0, 1024\n"
      "s_add_u32 s41, s41, s43\n"
      "s_mov_b64 s[0:1], s[10:11]\n"
      "s_movk_i32 s10, 0x4000\n"
      "s_mov_b32 s11, s7\n"
      "buffer_load_dwordx4 v3, s[4:7], s41 offen nt lds\n"
      "s_addk_i32 m0, 1024\n"
      "s_add_u32 s41, s41, s43\n"
      "buffer_load_dwordx4 v4, s[4:7], s41 offen nt lds\n"
      "s_addk_i32 m0, 1024\n"
      "s_add_u32 s41, s41, s43\n"
      "buffer_load_dwordx4 v3, s[4:7], s41 offen nt lds\n"
      "s_addk_i32 m0, 1024\n"
      "s_add_u32 s41, s41, s43\n"
      "buffer_load_dwordx4 v4, s[4:7], s41 offen nt lds\n"
      "s_mov_b32 m0, 94208\n"
      "s_add_u32 s41, s41, s43\n"
      "buffer_load_dwordx4 v7, s[8:11], s40 offen lds\n"
      "s_addk_i32 m0, 1056\n"
      "s_addk_i32 s40, 2048\n"
      "buffer_load_dwordx4 v7, s[8:11], s40 offen lds\n"
      "s_addk_i32 m0, 1056\n"
      "s_addk_i32 s40, 2048\n"
      "buffer_load_dwordx4 v7, s[8:11], s40 offen lds\n"
      "s_addk_i32 m0, 1056\n"
      "s_addk_i32 s40, 2048\n"
      "buffer_load_dwordx4 v7, s[8:11], s40 offen lds\n"
      "s_addk_i32 m0, 1056\n"
      "s_addk_i32 s40, 2048\n"
      "buffer_load_dwordx4 v7, s[8:11], s40 offen lds\n"
      "s_addk_i32 m0, 1056\n"
      "s_addk_i32 s40, 2048\n"
      "buffer_load_dwordx4 v7, s[8:11], s40 offen lds\n"
      "s_addk_i32 m0, 1056\n"
      "s_addk_i32 s40, 2048\n"
      "buffer_load_dwordx4 v7, s[8:11], s40 offen lds\n"
      "s_addk_i32 m0, 1056\n"
      "s_addk_i32 s40, 2048\n"
      "buffer_load_dwordx4 v7, s[8:11], s40 offen lds\n"
      "s_memrealtime s[58:59]\n"
      "s_movk_i32 m0, 8192\n"
      "s_lshr_b32 s55, s4, 7\n"
      "s_add_u32 s55, s55, s39\n"
      "s_and_b32 s55, s55, 7\n"
      "s_cmp_eq_u32 s55, 3\n"
      "s_cselect_b32 s56, 0, 13\n"
      "s_bfe_u32 s57, s2, 0x10003\n"
      "s_mul_i32 s57, s57, 6\n"
      "s_add_u32 s57, s57, 2\n"
      "s_waitcnt lgkmcnt(0)\n"
      "s_sub_u32 s37, s58, s62\n"
      "s_sub_u32 s63, s58, 10\n"
      "s_cmp_gt_i32 s37, 24\n"
      "s_cselect_b32 s63, s63, s62\n"
      "v_mov_b32 v8, s63\n"
      "s_add_u32 s57, s57, s63\n"
      ".p2alignl 5, 3212836864\n"
      "L_w1_%=:\n"
      "s_waitcnt lgkmcnt(0)\n"
      "s_sub_u32 s37, s58, s57\n"
      "s_memrealtime s[58:59]\n"
      "s_cmp_lt_i32 s37, 0\n"
      "s_cbranch_scc1 L_w1_%=\n"
      "s_add_u32 s57, s57, s56\n"
      "buffer_load_dwordx4 v3, s[4:7], s41 offen nt lds\n"
      "s_addk_i32 m0, 1024\n"
      "s_add_u32 s41, s41, s43\n"
      "buffer_load_dwordx4 v4, s[4:7], s41 offen nt lds\n"
      "s_addk_i32 m0, 1024\n"
      "s_add_u32 s41, s41, s43\n"
      "buffer_load_dwordx4 v3, s[4:7], s41 offen nt lds\n"
      "s_addk_i32 m0, 1024\n"
      "s_add_u32 s41, s41, s43\n"
      "buffer_load_dwordx4 v4, s[4:7], s41 offen nt lds\n"
      "s_addk_i32 m0, 1024\n"
      "s_add_u32 s41, s41, s43\n"
      "s_bitcmp1_b32 m0, 12\n"
      "s_cbranch_scc1 L_w1_%=\n"
      "s_cmp_le_u32 m0, 32768\n"
      "s_cbranch_scc1 L_w1_%=\n"
      "s_cmp_lg_u32 m0, 40960\n"
      "s_cbranch_scc1 L_nb0_%=\n"
      "s_barrier\n"
      "L_nb0_%=:\n"
      "v_add_u32 v6, 1, v6\n"
      "s_cmp_lg_u32 m0, 65536\n"
      "s_waitcnt vmcnt(32)\n"
      "ds_write2_b32 v5, v6, v8 offset1:3\n"
      "s_cbranch_scc1 L_w1_%=\n"
      "s_branch L_issued_%=\n"
      "L_extra_%=:\n"
      "s_getpc_b64 s[24:25]\n"
      "v_lshlrev_b32 v11, 6, v0\n"
      "s_andn2_b32 s24, s24, 0xfff\n"
      "global_load_dword v11, v11, s[24:25]\n"
      "s_lshl_b32 s37, s47, 13\n"
      "v_lshlrev_b32 v9, 21, v0\n"
      "v_lshl_add_u32 v10, v0, 7, s37\n"
      "s_waitcnt lgkmcnt(0)\n"
      "s_mov_b64 s[0:1], s[10:11]\n"
      "s_movk_i32 s10, 0x4000\n"
      "s_mov_b32 s11, s7\n"
      "buffer_load_dword v10, v10, s[8:11], 0 offen\n"
      "s_memrealtime s[58:59]\n"
      "s_bitcmp1_b32 s2, 3\n"
      "s_cselect_b64 s[4:5], s[16:17], s[4:5]\n"
      "s_bitcmp1_b32 s2, 4\n"
      "s_cselect_b64 s[24:25], s[12:13], s[4:5]\n"
      "s_mov_b64 s[26:27], s[6:7]\n"
      "buffer_load_dword v9, v9, s[24:27], 0 offen\n"
      "s_lshr_b32 s37, s4, 7\n"
      "s_sub_u32 s37, s28, s37\n"
      "s_add_u32 s37, s37, 3\n"
      "s_and_b32 s37, s37, 7\n"
      "s_cmp_eq_u32 s37, 7\n"
      "s_cselect_b32 s37, 0, s37\n"
      "s_cselect_b32 s44, 50, 38\n"
      "s_add_u32 s37, s37, s39\n"
      "s_sub_u32 s37, s37, 7\n"
      "s_lshl_b32 s37, s37, 7\n"
      "s_add_u32 s41, s41, s37\n"
      "s_sub_u32 s37, 64, s44\n"
      "s_mul_i32 s37, s37, s43\n"
      "s_add_u32 s41, s41, s37\n"
      "s_sub_u32 s60, s44, 30\n"
      "L_help_%=:\n"
      "buffer_load_dwordx4 v3, s[4:7], s41 offen lds\n"
      "s_add_u32 s41, s41, s43\n"
      "s_sub_u32 s44, s44, 1\n"
      "s_cmp_lg_u32 s44, s60\n"
      "s_waitcnt vmcnt(24)\n"
      "s_cbranch_scc1 L_help_%=\n"
      "s_cmp_eq_u32 s44, 0\n"
      "s_cbranch_scc1 L_helpd_%=\n"
      "s_mov_b32 s60, 0\n"
      "s_barrier\n"
      "s_branch L_help_%=\n"
      "L_helpd_%=:\n"
      "v_mov_b32 v6, 8\n"
      "s_waitcnt lgkmcnt(0)\n"
      "s_mov_b32 s63, s58\n"
      "v_mov_b32 v8, s58\n"
      "ds_write2_b32 v5, v6, v8 offset1:3\n"
      "L_issued_%=:\n"
      "ds_read_b32 v46, v5 offset:4\n"
      "s_lshr_b32 s37, s12, 7\n"
      "s_sub_u32 s37, s34, s37\n"
      "s_and_b32 s37, s37, 7\n"
      "s_lshl3_add_u32 s35, s35, s37\n"
      "s_mov_b64 s[20:21], s[14:15]\n"
      "s_mov_b32 s22, 0x800000\n"
      "s_mov_b32 s23, s7\n"
      "s_mul_i32 s46, s28, 33280\n"
      "s_add_u32 s48, s48, 1048576\n"
      "s_add_u32 s46, s46, s48\n"
      "s_lshl2_add_u32 s46, s29, s46\n"
      "s_and_b32 s37, s35, 3\n"
      "s_mul_i32 s37, s37, 1280\n"
      "s_mul_i32 s42, s38, 16640\n"
      "s_add_u32 s42, s42, s37\n"
      "s_add_u32 s42, s42, s48\n"
      "s_mul_i32 s52, s35, 16640\n"
      "s_lshl2_add_u32 s52, s38, s52\n"
      "s_add_u32 s52, s52, 2097152\n"
      "s_lshl_b32 s54, s28, 5\n"
      "s_add_u32 s54, s54, s29\n"
      "s_lshr_b32 s53, s54, 4\n"
      "s_mul_i32 s53, s53, 16640\n"
      "s_and_b32 s37, s29, 3\n"
      "s_mul_i32 s37, s37, 1280\n"
      "s_add_u32 s53, s53, s37\n"
      "s_add_u32 s53, s53, 2097152\n"
      "s_lshr_b32 s45, s54, 2\n"
      "s_lshl3_add_u32 s45, s45, s47\n"
      "s_lshl_b32 s45, s45, 11\n"
      "s_add_u32 s45, s45, 3145728\n"
      "s_lshl_b32 s54, s54, 3\n"
      "s_movk_i32 s40, 4096\n"
      "s_cmp_lt_u32 s29, 16\n"
      "s_cselect_b32 s50, 15, 0\n"
      "s_cmp_ge_u32 s29, 12\n"
      "s_cselect_b32 s37, 240, 0\n"
      "s_or_b32 s50, s50, s37\n"
      "s_mov_b32 s51, 0\n"
      "s_mov_b64 s[24:25], s[0:1]\n"
      "s_mov_b64 s[26:27], s[10:11]\n"
      "v_mul_u32_u24 v43, 1280, v0\n"
      "v_lshrrev_b32 v1, 2, v0\n"
      "v_mul_u32_u24 v1, 11472, v1\n"
      "v_add_u32 v44, v43, v1\n"
      "v_lshlrev_b32 v45, 2, v0\n"
      "v_and_b32 v1, 7, v0\n"
      "v_lshrrev_b32 v2, 4, v0\n"
      "v_lshlrev_b32 v1, 11, v1\n"
      "v_lshl_add_u32 v39, v2, 1, v1\n"
      "v_and_b32 v2, 31, v0\n"
      "v_lshlrev_b32 v42, 3, v2\n"
      "v_mov_b64 v[48:49], 0\n"
      "v_mov_b32 v40, 0\n"
      "v_mov_b64 v[58:59], 0\n"
      "v_mov_b64 v[60:61], 0\n"
      "v_mov_b64 v[52:53], 0\n"
      "v_mov_b32 v54, 0\n"
      "s_mov_b32 s44, 1048576\n"
      "s_add_u32 s57, s63, 256\n"
      "s_waitcnt lgkmcnt(0)\n"
      "v_readfirstlane_b32 s36, v46\n"
      "v_lshlrev_b32 v1, 4, v0\n"
      "v_mov_b32 v31, 0xff\n"
      "v_lshrrev_b32 v1, v1, v46\n"
      "v_and_b32 v56, 0xff, v1\n"
      "v_bfe_u32 v57, v1, 8, 8\n"
      "s_cmp_ge_u32 s29, 28\n"
      "s_cbranch_scc1 L_nofl_%=\n"
      "s_mov_b64 exec, 1\n"
      "buffer_atomic_or v48, v45, s[20:23], s42 offen sc0 sc1\n"
      "s_mov_b64 exec, 3\n"
      "v_lshlrev_b32 v1, 13, v0\n"
      "buffer_atomic_or v49, v1, s[20:23], s53 offen sc0 sc1\n"
      "s_mov_b64 exec, -1\n"
      "v_mov_b32 v6, 5\n"
      "s_waitcnt vmcnt(26)\n"
      "ds_write_b32 v5, v6\n"
      "v_mov_b32 v6, 6\n"
      "s_waitcnt vmcnt(18)\n"
      "ds_write_b32 v5, v6\n"
      "v_mov_b32 v6, 7\n"
      "s_waitcnt vmcnt(10)\n"
      "ds_write_b32 v5, v6\n"
      "v_mov_b32 v6, 8\n"
      "s_waitcnt vmcnt(2)\n"
      "ds_write_b32 v5, v6\n"
      "s_mov_b64 exec, s[50:51]\n"
      "buffer_atomic_swap v46, v44, s[20:23], s46 offen sc1\n"
      "L_nofl_%=:\n"
      "s_mov_b64 exec, 0xffff\n"
      "s_sleep 1\n"
      "L_timer_%=:\n"
      "s_waitcnt lgkmcnt(0)\n"
      "s_sub_u32 s37, s58, s57\n"
      "s_memrealtime s[58:59]\n"
      "s_cmp_lt_i32 s37, 0\n"
      "s_cbranch_scc1 L_timer_%=\n"
      "buffer_atomic_or v52, v45, s[20:23], s42 offen sc0 sc1\n"
      "s_sleep 4\n"
      "buffer_atomic_or v53, v45, s[20:23], s42 offen sc0 sc1\n"
      "s_sleep 4\n"
      "buffer_atomic_or v54, v45, s[20:23], s42 offen sc0 sc1\n"
      "s_waitcnt vmcnt(2)\n"
      "v_cmp_ne_u32 vcc, s36, v52\n"
      "s_cbranch_vccz L_1ok_%=\n"
      "s_waitcnt vmcnt(1)\n"
      "v_cmp_ne_u32 vcc, s36, v53\n"
      "v_mov_b32 v53, 0\n"
      "s_cbranch_vccz L_1ok_%=\n"
      "L_1poll_%=:\n"
      "buffer_atomic_or v53, v45, s[20:23], s42 offen sc0 sc1\n"
      "s_waitcnt vmcnt(1)\n"
      "v_cmp_ne_u32 vcc, s36, v54\n"
      "v_mov_b32 v54, 0\n"
      "s_cbranch_vccz L_1ok_%=\n"
      "buffer_atomic_or v54, v45, s[20:23], s42 offen sc0 sc1\n"
      "s_waitcnt vmcnt(1)\n"
      "v_cmp_ne_u32 vcc, s36, v53\n"
      "v_mov_b32 v53, 0\n"
      "s_cbranch_vccz L_1ok_%=\n"
      "s_sub_u32 s44, s44, 1\n"
      "s_cbranch_scc0 L_1poll_%=\n"
      "L_1ok_%=:\n"
      "s_barrier\n"
      "s_mov_b64 exec, -1\n"
      "s_mov_b32 s44, 1048576\n"
      "buffer_load_dwordx2 v[36:37], v42, s[20:23], s40 offen sc1\n"
      "s_lshr_b32 s35, s35, 1\n"
      "s_barrier\n"
      "buffer_load_short_d16_hi v40, v39, s[8:11], s54 offen\n"
      "s_waitcnt vmcnt(1)\n"
      "L_rpoll_%=:\n"
      "v_cmp_ne_u32 vcc, s36, v37\n"
      "s_cbranch_vccz L_rdone_%=\n"
      "buffer_load_dwordx2 v[36:37], v42, s[20:23], s40 offen sc1\n"
      "s_sub_u32 s44, s44, 1\n"
      "s_waitcnt vmcnt(0)\n"
      "s_cbranch_scc0 L_rpoll_%=\n"
      "L_rdone_%=:\n"
      "v_and_b32 v1, 1, v36\n"
      "v_and_b32 v2, 2, v36\n"
      "v_cmp_ne_u32 vcc, 0, v1\n"
      "v_cmp_ne_u32 s[50:51], 0, v2\n"
      "v_and_b32 v36, 0xffff0000, v36\n"
      "s_bcnt1_i32_b32 s37, vcc_lo\n"
      "s_cmp_ge_u32 s37, 16\n"
      "s_cselect_b32 s37, 0, -1\n"
      "s_xor_b32 s50, s50, s37\n"
      "s_bitcmp1_b32 s50, s35\n"
      "s_cselect_b32 s37, 0x2000, 0\n"
      "v_mov_b32 v6, s37\n"
      "s_add_u32 s52, s52, s37\n"
      "ds_write_b32 v5, v6 offset:8\n"
      "s_bitcmp1_b32 s50, s28\n"
      "s_cselect_b32 s37, 0x2000, 0\n"
      "s_add_u32 s53, s53, s37\n"
      "s_add_u32 s45, s45, s37\n"
      "s_mov_b32 s44, 1048576\n"
      "s_mov_b32 s61, s44\n"
      "s_mov_b64 exec, 15\n"
      "s_waitcnt vmcnt(0) lgkmcnt(0)\n"
      "buffer_atomic_swap v46, v43, s[20:23], s52 offen sc1\n"
      "s_mov_b64 exec, 0xffff\n"
      "s_barrier\n"
      "buffer_atomic_or v58, v45, s[20:23], s53 offen sc0 sc1\n"
      "s_sleep 3\n"
      "buffer_atomic_or v59, v45, s[20:23], s53 offen sc0 sc1\n"
      "s_sleep 3\n"
      "buffer_atomic_or v60, v45, s[20:23], s53 offen sc0 sc1\n"
      "s_sleep 3\n"
      "L_2poll_%=:\n"
      "buffer_atomic_or v61, v45, s[20:23], s53 offen sc0 sc1\n"
      "s_waitcnt vmcnt(3)\n"
      "v_cmp_ne_u32 vcc, s36, v58\n"
      "v_mov_b32 v58, 0\n"
      "s_cbranch_vccz L_2ok_%=\n"
      "buffer_atomic_or v58, v45, s[20:23], s53 offen sc0 sc1\n"
      "s_waitcnt vmcnt(3)\n"
      "v_cmp_ne_u32 vcc, s36, v59\n"
      "v_mov_b32 v59, 0\n"
      "s_cbranch_vccz L_2ok_%=\n"
      "buffer_atomic_or v59, v45, s[20:23], s53 offen sc0 sc1\n"
      "s_waitcnt vmcnt(3)\n"
      "v_cmp_ne_u32 vcc, s36, v60\n"
      "v_mov_b32 v60, 0\n"
      "s_cbranch_vccz L_2ok_%=\n"
      "buffer_atomic_or v60, v45, s[20:23], s53 offen sc0 sc1\n"
      "s_waitcnt vmcnt(3)\n"
      "v_cmp_ne_u32 vcc, s36, v61\n"
      "v_mov_b32 v61, 0\n"
      "s_cbranch_vccz L_2ok_%=\n"
      "s_sub_u32 s44, s44, 1\n"
      "s_cbranch_scc0 L_2poll_%=\n"
      "L_2ok_%=:\n"
      "s_mov_b64 exec, -1\n"
      "L_fpoll_%=:\n"
      "buffer_load_dwordx4 v[8:11], v7, s[20:23], s45 offen sc1\n"
      "buffer_load_dwordx4 v[12:15], v7, s[20:23], s45 offen offset:1024 sc1\n"
      "v_fmac_f32 v40, 0, v40\n"
      "s_cmp_eq_u32 s2, 0\n"
      "s_cbranch_scc1 L_epoch_%=\n"
      "s_waitcnt vmcnt(1)\n"
      "v_xor_b32 v8, v56, v8\n"
      "v_xor_b32 v10, v57, v10\n"
      "s_waitcnt vmcnt(0)\n"
      "L_epochd_%=:\n"
      "v_xor_b32 v12, v56, v12\n"
      "v_xor_b32 v14, v57, v14\n"
      "v_pk_add_f32 v[16:17], v[8:9], v[12:13]\n"
      "v_pk_add_f32 v[18:19], v[10:11], v[14:15]\n"
      "v_or3_b32 v8, v8, v10, v12\n"
      "v_bitop3_b32 v8, v8, v14, v31 bitop3:0xa8\n"
      "v_permlane32_swap_b32 v16, v18\n"
      "v_permlane32_swap_b32 v17, v19\n"
      "v_cmp_ne_u32 vcc, 0, v8\n"
      "v_pk_add_f32 v[16:17], v[16:17], v[18:19]\n"
      "s_sub_u32 s61, s61, 1\n"
      "s_cbranch_vccnz L_fretry_%=\n"
      "L_fdone_%=:\n"
      "v_permlane16_swap_b32 v16, v17\n"
      "v_add_f32 v8, v16, v17\n"
      "s_nop 1\n"
      "v_add_f32_dpp v8, v8, v8 row_ror:8 row_mask:0xf bank_mask:0xf\n"
      "v_cvt_pk_bf16_f32 v8, 0, v8\n"
      "v_mul_f32 v8, v8, v36\n"
      "v_cvt_pk_bf16_f32 v8, 0, v8\n"
      "v_add_f32 v8, v8, v40\n"
      "v_cvt_pk_bf16_f32 v8, v8, v8\n"
      "buffer_store_short v8, v39, s[24:27], s54 offen\n"
      "s_endpgm\n"
      "L_fretry_%=:\n"
      "s_cbranch_scc0 L_fpoll_%=\n"
      "s_branch L_fdone_%=\n"
      "L_epoch_%=:\n"
      "s_mov_b64 exec, 1\n"
      "buffer_atomic_swap v46, off, s[20:23], 0 sc0 sc1\n"
      "s_mov_b64 exec, -1\n"
      "s_mov_b32 s2, 256\n"
      "s_waitcnt vmcnt(1)\n"
      "v_xor_b32 v8, v56, v8\n"
      "v_xor_b32 v10, v57, v10\n"
      "s_branch L_epochd_%=\n"
      "L_compute_%=:\n"
      "v_and_b32 v0, 63, v0\n"
      "s_lshr_b32 s3, s3, 6\n"
      "s_sub_u32 s46, s3, 1\n"
      "s_waitcnt lgkmcnt(0)\n"
      "s_load_dword s36, s[14:15], 0\n"
      "s_lshr_b32 s37, s29, 2\n"
      "s_lshl3_add_u32 s31, s37, s3\n"
      "s_cmp_lg_u32 s31, 62\n"
      "s_cbranch_scc1 L_setup_%=\n"
      "s_setprio 3\n"
      "v_lshlrev_b32 v1, 5, v0\n"
      "s_lshl_b32 s37, s28, 11\n"
      "global_load_dwordx4 v[16:19], v1, s[18:19]\n"
      "global_load_dwordx4 v[20:23], v1, s[18:19] offset:16\n"
      "s_movk_i32 s10, 0x4000\n"
      "s_mov_b32 s11, s7\n"
      "buffer_load_dwordx4 v[8:11], v1, s[8:11], s37 offen\n"
      "buffer_load_dwordx4 v[12:15], v1, s[8:11], s37 offen offset:16\n"
      "s_setprio 0\n"
      "L_setup_%=:\n"
      "s_lshr_b32 s37, s12, 7\n"
      "s_sub_u32 s37, s34, s37\n"
      "s_and_b32 s37, s37, 7\n"
      "s_lshl3_add_u32 s35, s35, s37\n"
      "s_mov_b64 s[20:21], s[14:15]\n"
      "s_mov_b32 s22, 0x800000\n"
      "s_mov_b32 s23, s7\n"
      "v_lshlrev_b32 v28, 4, v0\n"
      "v_lshrrev_b32 v1, 4, v0\n"
      "v_bfe_u32 v2, v0, 2, 2\n"
      "v_bfe_u32 v3, v0, 3, 2\n"
      "v_and_b32 v4, 3, v0\n"
      "v_lshlrev_b32 v4, 3, v4\n"
      "v_lshl_add_u32 v4, v2, 7, v4\n"
      "v_xor_b32 v6, s46, v3\n"
      "v_lshl_add_u32 v6, v6, 5, v4\n"
      "v_lshl_add_u32 v24, v1, 10, v6\n"
      "v_and_b32 v2, 7, v0\n"
      "v_mul_u32_u24 v6, 1056, v2\n"
      "v_lshl_add_u32 v6, v1, 4, v6\n"
      "v_add_u32 v25, 94208, v6\n"
      "v_mul_u32_u24 v6, 480, v2\n"
      "v_add_u32 v6, 102656, v6\n"
      "v_lshl_add_u32 v26, v1, 4, v6\n"
      "v_and_b32 v7, 56, v0\n"
      "s_lshl_b32 s37, s46, 6\n"
      "v_add3_u32 v27, v6, v7, s37\n"
      "v_lshlrev_b32 v6, 4, v2\n"
      "v_and_b32 v7, 8, v0\n"
      "v_lshl_add_u32 v6, v7, 21, v6\n"
      "v_lshl_add_u32 v29, v1, 7, v6\n"
      "v_lshl_add_u32 v30, v1, 11, v6\n"
      "v_mov_b64 v[32:33], 0\n"
      "v_mov_b64 v[34:35], 0\n"
      "s_add_u32 s48, s48, 65536\n"
      "s_mul_i32 s37, s38, 7\n"
      "s_add_u32 s37, s37, s46\n"
      "s_lshr_b32 s40, s37, 1\n"
      "s_and_b32 s37, s37, 1\n"
      "s_lshl4_add_u32 s40, s40, s37\n"
      "s_lshl_b32 s40, s40, 10\n"
      "s_add_u32 s40, s40, s48\n"
      "s_add_u32 s49, s40, 4096\n"
      "s_lshl3_add_u32 s42, s39, s47\n"
      "s_lshl2_add_u32 s42, s42, s46\n"
      "s_lshl_b32 s42, s42, 9\n"
      "s_add_u32 s42, s42, s48\n"
      "s_lshl2_add_u32 s45, s35, s46\n"
      "s_lshl_b32 s45, s45, 7\n"
      "s_add_u32 s45, s45, s38\n"
      "s_lshl_b32 s45, s45, 7\n"
      "s_add_u32 s45, s45, 3145728\n"
      "s_mov_b32 s52, 0xbfb8aa3b\n"
      "s_mov_b32 s53, s52\n"
      "s_mov_b32 s44, 0\n"
      "s_waitcnt lgkmcnt(0)\n"
      "s_add_u32 s36, s36, 0x9e3779b1\n"
      "v_mov_b32 v46, s36\n"
      "ds_write2_b32 v5, v32, v46 offset1:1\n"
      "v_lshlrev_b32 v1, 4, v0\n"
      "v_mov_b32 v31, 0xff\n"
      "v_lshrrev_b32 v1, v1, v46\n"
      "v_and_b32 v56, 0xff, v1\n"
      "v_bfe_u32 v57, v1, 8, 8\n"
      "s_cmp_eq_u32 s3, 5\n"
      "s_waitcnt lgkmcnt(0)\n"
      "s_cbranch_scc1 L_wd_%=\n"
      "s_cmp_lt_u32 s29, 28\n"
      "s_barrier\n"
      "s_cbranch_scc1 L_wk_%=\n"
      "v_add_u32 v24, 65536, v24\n"
      "s_cmp_eq_u32 s3, 6\n"
      "s_cbranch_scc1 L_router_%=\n"
      "s_branch L_trig_%=\n"
      "L_wd_%=:\n"
      "v_lshrrev_b32 v1, 3, v0\n"
      "v_and_b32 v2, 2, v1\n"
      "v_xor_b32 v2, v2, v0\n"
      "v_and_b32 v2, 7, v2\n"
      "v_lshlrev_b32 v2, 4, v2\n"
      "v_lshl_add_u32 v3, v1, 11, v2\n"
      "v_xor_b32 v4, 64, v3\n"
      "s_cmp_ge_u32 s29, 28\n"
      "s_cselect_b32 s56, 0, 20\n"
      "s_cselect_b32 s57, 0, 205\n"
      "s_min_u32 s62, s56, 12\n"
      "s_mov_b64 s[4:5], s[12:13]\n"
      "s_mul_i32 s41, s38, 458752\n"
      "s_lshl_b32 s37, s35, 7\n"
      "s_add_u32 s41, s41, s37\n"
      "s_movk_i32 s43, 0x4000\n"
      "s_mov_b32 m0, 65536\n"
      "s_barrier\n"
      "L_baton_%=:\n"
      "s_sleep 1\n"
      "ds_read2_b32 v[6:7], v5 offset1:3\n"
      "s_waitcnt lgkmcnt(0)\n"
      "v_cmp_gt_u32 vcc, 4, v6\n"
      "s_cbranch_vccnz L_baton_%=\n"
      "s_memrealtime s[58:59]\n"
      "v_readfirstlane_b32 s37, v7\n"
      "s_setprio 2\n"
      "s_add_u32 s57, s57, s37\n"
      ".p2alignl 5, 3212836864\n"
      "L_d_%=:\n"
      "s_waitcnt lgkmcnt(0)\n"
      "s_sub_u32 s37, s58, s57\n"
      "s_memrealtime s[58:59]\n"
      "s_cmp_lt_i32 s37, 0\n"
      "s_cbranch_scc1 L_d_%=\n"
      "s_add_u32 s57, s57, s56\n"
      "s_sub_i32 s56, s56, 4\n"
      "s_max_i32 s56, s56, s62\n"
      "buffer_load_dwordx4 v3, s[4:7], s41 offen nt lds\n"
      "s_addk_i32 m0, 1024\n"
      "s_add_u32 s41, s41, s43\n"
      "buffer_load_dwordx4 v4, s[4:7], s41 offen nt lds\n"
      "s_addk_i32 m0, 1024\n"
      "s_add_u32 s41, s41, s43\n"
      "buffer_load_dwordx4 v3, s[4:7], s41 offen nt lds\n"
      "s_addk_i32 m0, 1024\n"
      "s_add_u32 s41, s41, s43\n"
      "buffer_load_dwordx4 v4, s[4:7], s41 offen nt lds\n"
      "s_addk_i32 m0, 1024\n"
      "s_add_u32 s41, s41, s43\n"
      "s_cmp_lt_u32 m0, 94208\n"
      "s_cbranch_scc1 L_d_%=\n"
      "s_setprio 0\n"
      "s_branch L_trig_%=\n"
      "L_wk_%=:\n"
      "s_cmp_gt_u32 s3, 4\n"
      "s_cbranch_scc1 L_trig_%=\n"
      ".p2alignl 5, 3212836864\n"
      "L_p1_%=:\n"
      "ds_read_b32 v6, v5\n"
      "ds_read_b64_tr_b16 v[36:37], v24\n"
      "ds_read_b64_tr_b16 v[38:39], v24 offset:512\n"
      "ds_read_b128 v[44:47], v25\n"
      "ds_read_b64_tr_b16 v[40:41], v24 offset:4096\n"
      "ds_read_b64_tr_b16 v[42:43], v24 offset:4608\n"
      "ds_read_b128 v[48:51], v25 offset:64\n"
      "s_waitcnt lgkmcnt(6)\n"
      "v_cmp_ge_u32 vcc, s44, v6\n"
      "s_cbranch_vccnz L_p1_%=\n"
      "v_add_u32 v24, 0x2000, v24\n"
      "v_add_u32 v25, 0x80, v25\n"
      "s_add_u32 s44, s44, 1\n"
      "s_cmp_lg_u32 s44, 8\n"
      "s_waitcnt lgkmcnt(3)\n"
      "v_mfma_f32_16x16x32_bf16 v[32:35], v[36:39], v[44:47], v[32:35]\n"
      "s_waitcnt lgkmcnt(0)\n"
      "v_mfma_f32_16x16x32_bf16 v[32:35], v[40:43], v[48:51], v[32:35]\n"
      "s_cbranch_scc1 L_p1_%=\n"
      "s_nop 6\n"
      "v_bfi_b32 v32, v31, v56, v32\n"
      "v_bfi_b32 v34, v31, v57, v34\n"
      "buffer_store_dwordx4 v[32:35], v29, s[20:23], s42 offen sc1\n"
      "L_trig_%=:\n"
      "s_mov_b32 s44, 1048576\n"
      "s_barrier\n"
      "L_g_%=:\n"
      "buffer_load_dwordx4 v[8:11], v28, s[20:23], s40 offen sc1\n"
      "buffer_load_dwordx4 v[16:19], v28, s[20:23], s49 offen sc1\n"
      "s_cmp_gt_u32 s3, 4\n"
      "s_cbranch_scc1 L_nsl_%=\n"
      "s_sleep 2\n"
      "L_nsl_%=:\n"
      "buffer_load_dwordx4 v[12:15], v28, s[20:23], s40 offen offset:2048 sc1\n"
      "buffer_load_dwordx4 v[20:23], v28, s[20:23], s49 offen offset:2048 sc1\n"
      "s_cmp_lg_u32 s44, 1048576\n"
      "s_cbranch_scc1 L_gskip_%=\n"
      "s_cmp_gt_u32 s3, 4\n"
      "s_waitcnt vmcnt(4)\n"
      "s_barrier\n"
      "s_cbranch_scc1 L_gskip_%=\n"
      "ds_read_b64_tr_b16 v[60:61], v24 offset:0\n"
      "ds_read_b64_tr_b16 v[62:63], v24 offset:512\n"
      "ds_read_b64_tr_b16 v[64:65], v24 offset:4096\n"
      "ds_read_b64_tr_b16 v[66:67], v24 offset:4608\n"
      "ds_read_b64_tr_b16 v[68:69], v24 offset:8192\n"
      "ds_read_b64_tr_b16 v[70:71], v24 offset:8704\n"
      "ds_read_b64_tr_b16 v[72:73], v24 offset:12288\n"
      "ds_read_b64_tr_b16 v[74:75], v24 offset:12800\n"
      "ds_read_b64_tr_b16 v[76:77], v24 offset:16384\n"
      "ds_read_b64_tr_b16 v[78:79], v24 offset:16896\n"
      "ds_read_b64_tr_b16 v[80:81], v24 offset:20480\n"
      "ds_read_b64_tr_b16 v[82:83], v24 offset:20992\n"
      "ds_read_b64_tr_b16 v[84:85], v24 offset:24576\n"
      "ds_read_b64_tr_b16 v[86:87], v24 offset:25088\n"
      "L_gskip_%=:\n"
      "s_waitcnt vmcnt(2)\n"
      "v_xor_b32 v8, v56, v8\n"
      "v_xor_b32 v16, v56, v16\n"
      "v_xor_b32 v10, v57, v10\n"
      "v_xor_b32 v18, v57, v18\n"
      "v_pk_add_f32 v[2:3], v[8:9], v[16:17]\n"
      "v_pk_add_f32 v[40:41], v[10:11], v[18:19]\n"
      "v_pk_mul_f32 v[36:37], v[2:3], s[52:53]\n"
      "v_pk_mul_f32 v[38:39], v[40:41], s[52:53]\n"
      "v_exp_f32 v36, v36\n"
      "v_exp_f32 v37, v37\n"
      "v_exp_f32 v38, v38\n"
      "v_exp_f32 v39, v39\n"
      "v_pk_add_f32 v[36:37], v[36:37], 1.0 op_sel_hi:[1,0]\n"
      "v_pk_add_f32 v[38:39], v[38:39], 1.0 op_sel_hi:[1,0]\n"
      "v_rcp_f32 v36, v36\n"
      "v_rcp_f32 v37, v37\n"
      "v_rcp_f32 v38, v38\n"
      "v_rcp_f32 v39, v39\n"
      "v_pk_mul_f32 v[2:3], v[2:3], v[36:37]\n"
      "v_pk_mul_f32 v[40:41], v[40:41], v[38:39]\n"
      "v_or3_b32 v1, v8, v10, v16\n"
      "s_waitcnt vmcnt(0)\n"
      "v_xor_b32 v12, v56, v12\n"
      "v_xor_b32 v20, v56, v20\n"
      "v_xor_b32 v14, v57, v14\n"
      "v_xor_b32 v22, v57, v22\n"
      "v_pk_add_f32 v[36:37], v[12:13], v[20:21]\n"
      "v_pk_add_f32 v[38:39], v[14:15], v[22:23]\n"
      "v_pk_mul_f32 v[2:3], v[2:3], v[36:37]\n"
      "v_pk_mul_f32 v[40:41], v[40:41], v[38:39]\n"
      "v_cvt_pk_bf16_f32 v2, v2, v3\n"
      "v_cvt_pk_bf16_f32 v3, v40, v41\n"
      "ds_write_b64 v27, v[2:3]\n"
      "v_or3_b32 v6, v12, v14, v18\n"
      "v_or3_b32 v1, v1, v20, v22\n"
      "v_bitop3_b32 v1, v1, v6, v31 bitop3:0xa8\n"
      "v_cmp_ne_u32 vcc, 0, v1\n"
      "s_cmp_gt_u32 s3, 4\n"
      "s_cbranch_vccnz L_gretry_%=\n"
      "L_gdone_%=:\n"
      "s_waitcnt lgkmcnt(0)\n"
      "s_barrier\n"
      "s_cbranch_scc1 L_cend_%=\n"
      "ds_read_b128 v[8:11], v26 offset:0\n"
      "ds_read_b128 v[12:15], v26 offset:64\n"
      "ds_read_b128 v[16:19], v26 offset:128\n"
      "ds_read_b128 v[20:23], v26 offset:192\n"
      "ds_read_b128 v[36:39], v26 offset:256\n"
      "ds_read_b128 v[40:43], v26 offset:320\n"
      "ds_read_b128 v[44:47], v26 offset:384\n"
      "ds_read_b32 v6, v5 offset:8\n"
      "s_waitcnt lgkmcnt(7)\n"
      "v_mfma_f32_16x16x32_bf16 v[32:35], v[60:63], v[8:11], 0\n"
      "s_waitcnt lgkmcnt(6)\n"
      "v_mfma_f32_16x16x32_bf16 v[32:35], v[64:67], v[12:15], v[32:35]\n"
      "s_waitcnt lgkmcnt(5)\n"
      "v_mfma_f32_16x16x32_bf16 v[32:35], v[68:71], v[16:19], v[32:35]\n"
      "s_waitcnt lgkmcnt(4)\n"
      "v_mfma_f32_16x16x32_bf16 v[32:35], v[72:75], v[20:23], v[32:35]\n"
      "s_waitcnt lgkmcnt(3)\n"
      "v_mfma_f32_16x16x32_bf16 v[32:35], v[76:79], v[36:39], v[32:35]\n"
      "s_waitcnt lgkmcnt(2)\n"
      "v_mfma_f32_16x16x32_bf16 v[32:35], v[80:83], v[40:43], v[32:35]\n"
      "s_waitcnt lgkmcnt(1)\n"
      "v_mfma_f32_16x16x32_bf16 v[32:35], v[84:87], v[44:47], v[32:35]\n"
      "s_waitcnt lgkmcnt(0)\n"
      "v_add_u32 v30, v30, v6\n"
      "s_nop 5\n"
      "v_bfi_b32 v32, v31, v56, v32\n"
      "v_bfi_b32 v34, v31, v57, v34\n"
      "buffer_store_dwordx4 v[32:35], v30, s[20:23], s45 offen sc1\n"
      "L_cend_%=:\n"
      "s_endpgm\n"
      "L_gretry_%=:\n"
      "s_sub_u32 s44, s44, 1\n"
      "s_cbranch_scc0 L_g_%=\n"
      "s_cmp_gt_u32 s3, 4\n"
      "s_branch L_gdone_%=\n"
      "L_router_%=:\n"
      "s_mul_i32 s24, s2, 16640\n"
      "s_add_u32 s24, s24, -1105920\n"
      "s_add_u32 s24, s24, s14\n"
      "s_addc_u32 s25, s15, 0\n"
      "s_getreg_b32 s50, hwreg(HW_REG_XCC_ID, 2, 1)\n"
      "s_load_dword s26, s[24:25], 0\n"
      "s_load_dword s27, s[24:25], 0x2000\n"
      "s_mul_i32 s50, s50, 3\n"
      "s_waitcnt lgkmcnt(1)\n"
      "s_nop 7\n"
      "s_getreg_b32 s37, hwreg(HW_REG_IB_STS, 8, 1)\n"
      "s_xor_b32 s50, s50, s37\n"
      "s_waitcnt vmcnt(0)\n"
      "v_dot2c_f32_bf16 v32, v8, v16\n"
      "v_dot2c_f32_bf16 v32, v9, v17\n"
      "v_dot2c_f32_bf16 v32, v10, v18\n"
      "v_dot2c_f32_bf16 v32, v11, v19\n"
      "v_dot2c_f32_bf16 v32, v12, v20\n"
      "v_dot2c_f32_bf16 v32, v13, v21\n"
      "v_dot2c_f32_bf16 v32, v14, v22\n"
      "v_dot2c_f32_bf16 v32, v15, v23\n"
      "s_nop 1\n"
      "s_nop 1\n"
      "v_add_f32_dpp v32, v32, v32 row_ror:1 row_mask:0xf bank_mask:0xf\n"
      "s_nop 1\n"
      "v_add_f32_dpp v32, v32, v32 row_ror:2 row_mask:0xf bank_mask:0xf\n"
      "s_nop 1\n"
      "v_add_f32_dpp v32, v32, v32 row_ror:4 row_mask:0xf bank_mask:0xf\n"
      "s_nop 1\n"
      "v_add_f32_dpp v32, v32, v32 row_ror:8 row_mask:0xf bank_mask:0xf\n"
      "v_mov_b32 v3, v32\n"
      "s_nop 1\n"
      "v_permlane16_swap_b32 v32, v3\n"
      "v_add_f32 v32, v32, v3\n"
      "v_mov_b32 v3, v32\n"
      "s_nop 1\n"
      "v_permlane32_swap_b32 v32, v3\n"
      "v_add_f32 v32, v32, v3\n"
      "v_cvt_pk_bf16_f32 v32, v32, v32\n"
      "v_and_b32 v32, 0xffff0000, v32\n"
      "v_mul_f32 v32, s52, v32\n"
      "v_exp_f32 v32, v32\n"
      "s_lshl_b32 s37, s2, 3\n"
      "v_add_f32 v32, 1.0, v32\n"
      "v_rcp_f32 v32, v32\n"
      "v_mov_b32 v33, s36\n"
      "v_cvt_pk_bf16_f32 v32, v32, v32\n"
      "v_and_b32 v32, 0xffff0000, v32\n"
      "v_or_b32 v32, s50, v32\n"
      "s_mov_b64 exec, 1\n"
      "buffer_store_dwordx2 v[32:33], off, s[20:23], s37 offset:2304 sc1\n"
      "s_mov_b64 exec, -1\n"
      "s_branch L_trig_%=\n"
      :
      : "{s[0:1]}"(__builtin_amdgcn_kernarg_segment_ptr()), "{s2}"(__builtin_amdgcn_workgroup_id_x()),
        "{v0}"(__builtin_amdgcn_workitem_id_x())
      : "v1", "v2", "v3", "v4", "v5", "v6", "v7", "v8", "v9", "v10", "v11", "v12", "v13", "v14", "v15", "v16",
        "v17", "v18", "v19", "v20", "v21", "v22", "v23", "v24", "v25", "v26", "v27", "v28", "v29", "v30", "v31", "v32",
        "v33", "v34", "v35", "v36", "v37", "v38", "v39", "v40", "v41", "v42", "v43", "v44", "v45", "v46", "v47", "v48",
        "v49", "v50", "v51", "v52", "v53", "v54", "v55", "v56", "v57", "v58", "v59", "v60", "v61", "v62", "v63", "v64",
        "v65", "v66", "v67", "v68", "v69", "v70", "v71", "v72", "v73", "v74", "v75", "v76", "v77", "v78", "v79", "v80",
        "v81", "v82", "v83", "v84", "v85", "v86", "v87", "s3", "s4", "s5", "s6", "s7", "s8", "s9", "s10", "s11",
        "s12", "s13", "s14", "s15", "s16", "s17", "s18", "s19", "s20", "s21", "s22", "s23", "s24", "s25", "s26", "s27",
        "s28", "s29", "s31", "s33", "s34", "s35", "s36", "s37", "s38", "s39", "s40", "s41", "s42", "s43", "s44", "s45",
        "s46", "s47", "s48", "s49", "s50", "s51", "s52", "s53", "s54", "s55", "s56", "s57", "s58", "s59", "s60", "s61",
        "s62", "s63", "vcc", "scc", "memory");
  asm volatile("" ::"s"((uint32_t)(uintptr_t)(__attribute__((address_space(3))) char*)lds));
}

extern "C" void kernel_launch(void* const* d_in, const int* in_sizes, int n_in, void* d_out, int out_size,
                              void* d_ws, size_t ws_size, hipStream_t stream) {
  const uint32_t f = 0x00020000u;
  mod_decode_kernel<<<256, 512, 0, stream>>>(d_in[2], 1024u * 3584u * 2u, f, d_in[0], d_out, d_in[4], d_ws,
      d_in[3], d_in[1]);
}
```
